```python
import torch

def _softmax(x, axis=-1):
    return torch.softmax(x, dim=axis)
def _gelu(x):
    return torch.nn.functional.gelu(x, approximate="tanh")
def _silu(x):
    return torch.nn.functional.silu(x)
def _sigmoid(x):
    return torch.sigmoid(x)
def _layernorm(x, axis=-1, eps=1e-5):
    m = x.mean(dim=axis, keepdim=True)
    v = x.var(dim=axis, keepdim=True, unbiased=False)
    return (x - m) / torch.sqrt(v + eps)
def _scatter_add(tgt, idx, src, axis=0):
    return tgt.clone().index_add_(axis, idx, src)
def _segment_sum(src, seg, n):
    out = torch.zeros((n,) + src.shape[1:], dtype=src.dtype)
    return out.index_add_(0, seg, src)
def _pad(x, pw):
    flat = tuple(v for pair in reversed(pw) for v in pair)
    return torch.nn.functional.pad(x, flat)

kernel_name = 'standalone__fm_second_order__recsys'

def setup_inputs():

    _w = (lambda a: (lambda t: t.to(torch.bfloat16) if t.is_floating_point() else t)(torch.as_tensor(a)))
    B, F, D, V = 16384, 26, 128, min(100000, 100000)
    return {
        "idx": _w((lambda lo, hi, *s: torch.randint(lo, hi, s))(0, V, B, F).to(torch.int64)),
        "table": _w((lambda *s: torch.randn(*s, dtype=torch.bfloat16) * 0.05)(V, D)),
    }

def reference(idx, table):
    emb = table[idx]
    sum_sq = emb.sum(axis=1) ** 2
    sq_sum = (emb ** 2).sum(axis=1)
    return 0.5 * (sum_sq - sq_sum).sum(axis=-1)
```

```cpp
#include <hip/hip_runtime.h>
#include <hip/hip_bf16.h>
#include <stdint.h>

typedef __attribute__((ext_vector_type(4))) unsigned int u32x4;

constexpr int F = 26;
constexpr int D = 128;

template <int N>
__device__ __forceinline__ unsigned row_bcast(unsigned v) {
    return (unsigned)__builtin_amdgcn_update_dpp(0, (int)v, 0x150 + N, 0xf, 0xf, false);
}

__device__ __forceinline__ float lo_f(unsigned u) { return __uint_as_float(u << 16); }
__device__ __forceinline__ float hi_f(unsigned u) { return __uint_as_float(u & 0xffff0000u); }

template <int I>
struct Loader {
    static __device__ __forceinline__ void run(u32x4 (&r)[F], const char* table, unsigned o0, unsigned o1, unsigned coff) {
        unsigned off = (I < 16 ? row_bcast<I & 15>(o0) : row_bcast<I & 15>(o1)) + coff;
        r[I] = *reinterpret_cast<const u32x4*>(table + off);
        Loader<I + 1>::run(r, table, o0, o1, coff);
    }
};
template <>
struct Loader<F> {
    static __device__ __forceinline__ void run(u32x4 (&)[F], const char*, unsigned, unsigned, unsigned) {}
};

__global__ __launch_bounds__(256) void fm_kernel(const int* __restrict__ idx, const char* __restrict__ table,
                                                 __hip_bfloat16* __restrict__ out, int B, int V) {
    const int lane = threadIdx.x & 63;
    const int wave = (blockIdx.x * 256 + threadIdx.x) >> 6;
    const int g = lane >> 4, c = lane & 15;
    int b = wave * 4 + g;
    const bool valid = b < B;
    if (!valid) b = B - 1;
    const int* ip = idx + (size_t)b * F;
    int i0 = ip[c];
    int i1 = ip[c < F - 16 ? 16 + c : 0];
    i0 = i0 < 0 ? i0 + V : i0;
    i1 = i1 < 0 ? i1 + V : i1;
    unsigned o0 = min((unsigned)i0, (unsigned)(V - 1)) * (D * 2);
    unsigned o1 = min((unsigned)i1, (unsigned)(V - 1)) * (D * 2);
    u32x4 r[F];
    Loader<0>::run(r, table, o0, o1, (unsigned)c * 16);
    float s[8];
#pragma unroll
    for (int j = 0; j < 8; ++j) s[j] = 0.f;
    float q0 = 0.f, q1 = 0.f;
#pragma unroll
    for (int f = 0; f < F; ++f) {
#pragma unroll
        for (int k = 0; k < 4; ++k) {
            float a = lo_f(r[f][k]), h = hi_f(r[f][k]);
            s[2 * k] += a;
            s[2 * k + 1] += h;
            q0 = fmaf(a, a, q0);
            q1 = fmaf(h, h, q1);
        }
    }
    float t = 0.f;
#pragma unroll
    for (int j = 0; j < 8; ++j) t = fmaf(s[j], s[j], t);
    t -= (q0 + q1);
    t += __shfl_xor(t, 1, 64);
    t += __shfl_xor(t, 2, 64);
    t += __shfl_xor(t, 4, 64);
    t += __shfl_xor(t, 8, 64);
    if (c == 0 && valid) out[b] = __float2bfloat16(0.5f * t);
}

extern "C" void kernel_launch(void* const* d_in, const int* in_sizes, int n_in,
                              void* d_out, int out_size, void* d_ws, size_t ws_size,
                              hipStream_t stream) {
    const int* idx = (const int*)d_in[0];
    const char* table = (const char*)d_in[1];
    __hip_bfloat16* out = (__hip_bfloat16*)d_out;
    const int B = out_size;
    const int V = in_sizes[1] / D;
    const int blocks = (B + 15) / 16;
    fm_kernel<<<blocks, 256, 0, stream>>>(idx, table, out, B, V);
}
```

```cpp
#include <hip/hip_runtime.h>
#include <hip/hip_bf16.h>
#include <stdint.h>
typedef __attribute__((ext_vector_type(4))) unsigned int u32x4;
typedef __attribute__((ext_vector_type(2))) int i32x2;
constexpr int F = 26;
__device__ __forceinline__ float lo_f(unsigned u) { return __uint_as_float(u << 16); }
__device__ __forceinline__ float hi_f(unsigned u) { return __uint_as_float(u & 0xffff0000u); }
template <int SEG, int DEPTH, int NT>
__global__ __launch_bounds__(256) void fm_kernel(const int* __restrict__ idx, const char* __restrict__ table, __hip_bfloat16* __restrict__ out, int B, int V) {
    constexpr int PASSES = 256 / SEG, LPS = SEG / 16, SPW = 64 / LPS;
    const int lane = threadIdx.x & 63, w = threadIdx.x >> 6;
    int b = (blockIdx.x * 4 + w) * SPW + lane / LPS;
    const bool valid = b < B; if (!valid) b = B - 1;
    const int c = lane % LPS;
    unsigned off[F];
    const i32x2* ip = (const i32x2*)(idx + (size_t)b * F);
#pragma unroll
    for (int j = 0; j < F / 2; ++j) { i32x2 v = ip[j]; off[2 * j] = min((unsigned)v.x, (unsigned)V - 1) * 256u + c * 16; off[2 * j + 1] = min((unsigned)v.y, (unsigned)V - 1) * 256u + c * 16; }
    float t = 0.f;
    u32x4 r[DEPTH];
    float s[8]; float q0 = 0.f, q1 = 0.f;
#pragma unroll
    for (int j = 0; j < 8; ++j) s[j] = 0.f;
    constexpr int N = F * PASSES;
    __builtin_amdgcn_sched_barrier(0);
#pragma unroll
    for (int i = 0; i < N + DEPTH; ++i) {
        if (i >= DEPTH) {
            const int j = i - DEPTH;
            u32x4 x = r[j % DEPTH];
#pragma unroll
            for (int kk = 0; kk < 4; ++kk) { float a = lo_f(x[kk]), h = hi_f(x[kk]); s[2 * kk] += a; s[2 * kk + 1] += h; q0 = fmaf(a, a, q0); q1 = fmaf(h, h, q1); }
            if (j % F == F - 1) {
#pragma unroll
                for (int jj = 0; jj < 8; ++jj) { t = fmaf(s[jj], s[jj], t); s[jj] = 0.f; }
            }
        }
        if (i < N) { const u32x4* p = reinterpret_cast<const u32x4*>(table + off[i % F] + (i / F) * SEG); r[i % DEPTH] = NT ? __builtin_nontemporal_load(p) : *p; }
        __builtin_amdgcn_sched_barrier(0);
    }
    t -= q0 + q1;
#pragma unroll
    for (int m = 1; m < LPS; m <<= 1) t += __shfl_xor(t, m, 64);
    if (c == 0 && valid) out[b] = __float2bfloat16(0.5f * t);
}
extern "C" void kernel_launch(void* const* d_in, const int* in_sizes, int n_in, void* d_out, int out_size, void* d_ws, size_t ws_size, hipStream_t stream) {
    const int B = out_size; const int V = in_sizes[1] / 128;
    constexpr int SPB = 4 * 64 / (128 / 16);
    fm_kernel<128, 8, 0><<<(B + SPB - 1) / SPB, 256, 0, stream>>>((const int*)d_in[0], (const char*)d_in[1], (__hip_bfloat16*)d_out, B, V);
}
```

```cpp
#include <hip/hip_runtime.h>
#include <hip/hip_bf16.h>
#include <stdint.h>
typedef __attribute__((ext_vector_type(4))) unsigned int u32x4;
typedef __attribute__((ext_vector_type(2))) int i32x2;
constexpr int F = 26;
__device__ __forceinline__ float lo_f(unsigned u) { return __uint_as_float(u << 16); }
__device__ __forceinline__ float hi_f(unsigned u) { return __uint_as_float(u & 0xffff0000u); }
template <int SEG, int XS, int DEPTH>
__global__ __launch_bounds__(256) void fm_kernel(const int* __restrict__ idx, const char* __restrict__ table, float* __restrict__ part, int B, int V, int nblk) {
    constexpr int LPS = SEG / 16, SPW = 64 / LPS;
    const int lane = threadIdx.x & 63, w = threadIdx.x >> 6;
    const int rep = blockIdx.x / nblk; const int bid = blockIdx.x % nblk;
    const int xcd = (bid + rep * 3) & 7, nb = bid >> 3;
    const int cp0 = xcd % XS, sg = xcd / XS;
    const int b = sg * (B * XS / 8) + (nb * 4 + w) * SPW + lane / LPS;
    const int c = lane % LPS;
    unsigned off[F];
    const i32x2* ip = (const i32x2*)(idx + (size_t)b * F);
#pragma unroll
    for (int j = 0; j < F / 2; ++j) { i32x2 v = ip[j]; off[2 * j] = min((unsigned)v.x, (unsigned)V - 1) * 256u + c * 16 + cp0 * SEG; off[2 * j + 1] = min((unsigned)v.y, (unsigned)V - 1) * 256u + c * 16 + cp0 * SEG; }
    float t = 0.f;
    u32x4 r[DEPTH];
    float s[8]; float q0 = 0.f, q1 = 0.f;
#pragma unroll
    for (int j = 0; j < 8; ++j) s[j] = 0.f;
    constexpr int N = F;
    __builtin_amdgcn_sched_barrier(0);
#pragma unroll
    for (int i = 0; i < N + DEPTH; ++i) {
        if (i >= DEPTH) {
            const int j = i - DEPTH;
            u32x4 x = r[j % DEPTH];
#pragma unroll
            for (int kk = 0; kk < 4; ++kk) { float a = lo_f(x[kk]), h = hi_f(x[kk]); s[2 * kk] += a; s[2 * kk + 1] += h; q0 = fmaf(a, a, q0); q1 = fmaf(h, h, q1); }
        }
        if (i < N) { const u32x4* p = reinterpret_cast<const u32x4*>(table + off[i]); r[i % DEPTH] = *p; }
        __builtin_amdgcn_sched_barrier(0);
    }
#pragma unroll
    for (int jj = 0; jj < 8; ++jj) t = fmaf(s[jj], s[jj], t);
    t -= q0 + q1;
#pragma unroll
    for (int m = 1; m < LPS; m <<= 1) t += __shfl_xor(t, m, 64);
    if (c == 0) part[(rep * XS + cp0) * B + b] = t;
}
__global__ void combine(const float* __restrict__ part, __hip_bfloat16* __restrict__ out, int B, int n, float scale) {
    int b = blockIdx.x * 256 + threadIdx.x; if (b >= B) return;
    float t = 0.f; for (int i = 0; i < n; ++i) t += part[i * B + b];
    out[b] = __float2bfloat16(scale * t);
}
extern "C" void kernel_launch(void* const* d_in, const int* in_sizes, int n_in, void* d_out, int out_size, void* d_ws, size_t ws_size, hipStream_t stream) {
    const int B = out_size; const int V = in_sizes[1] / 128;
    fm_kernel<128, 2, 8><<<1024 * 1, 256, 0, stream>>>((const int*)d_in[0], (const char*)d_in[1], (float*)d_ws, B, V, 1024);
    combine<<<(B + 255) / 256, 256, 0, stream>>>((const float*)d_ws, (__hip_bfloat16*)d_out, B, 2 * 1, 0.5f / 1);
}
```

```cpp
#include <hip/hip_runtime.h>
#include <hip/hip_bf16.h>
#include <stdint.h>
typedef __attribute__((ext_vector_type(4))) unsigned int u32x4;
typedef __attribute__((ext_vector_type(2))) int i32x2;
constexpr int F = 26;
__device__ __forceinline__ float lo_f(unsigned u) { return __uint_as_float(u << 16); }
__device__ __forceinline__ float hi_f(unsigned u) { return __uint_as_float(u & 0xffff0000u); }
template <int SEG, int DEPTH, int NT>
__global__ __launch_bounds__(256) void fm_kernel(const int* __restrict__ idx, const char* __restrict__ table, __hip_bfloat16* __restrict__ out, int B, int V) {
    constexpr int PASSES = 256 / SEG, LPS = SEG / 16, SPW = 64 / LPS;
    const int lane = threadIdx.x & 63, w = threadIdx.x >> 6;
    int b = (blockIdx.x * 4 + w) * SPW + lane / LPS;
    const bool valid = b < B; if (!valid) b = B - 1;
    const int c = lane % LPS;
    unsigned off[F];
    const i32x2* ip = (const i32x2*)(idx + (size_t)b * F);
    unsigned pf;
    { const char* pa = table + min((unsigned)(lane & 31) << 20, (unsigned)V * 256u - 4u);
      pf = *(const unsigned short*)pa; }
#pragma unroll
    for (int j = 0; j < F / 2; ++j) { i32x2 v = ip[j]; off[2 * j] = min((unsigned)v.x, (unsigned)V - 1) * 256u + c * 16; off[2 * j + 1] = min((unsigned)v.y, (unsigned)V - 1) * 256u + c * 16; }
    float t = 0.f;
    u32x4 r[DEPTH];
    float s[8]; float q0 = 0.f, q1 = 0.f;
#pragma unroll
    for (int j = 0; j < 8; ++j) s[j] = 0.f;
    constexpr int N = F * PASSES;
    __builtin_amdgcn_sched_barrier(0);
#pragma unroll
    for (int i = 0; i < N + DEPTH; ++i) {
        if (i >= DEPTH) {
            const int j = i - DEPTH;
            u32x4 x = r[j % DEPTH];
#pragma unroll
            for (int kk = 0; kk < 4; ++kk) { float a = lo_f(x[kk]), h = hi_f(x[kk]); s[2 * kk] += a; s[2 * kk + 1] += h; q0 = fmaf(a, a, q0); q1 = fmaf(h, h, q1); }
            if (j % F == F - 1) {
#pragma unroll
                for (int jj = 0; jj < 8; ++jj) { t = fmaf(s[jj], s[jj], t); s[jj] = 0.f; }
            }
        }
        if (i < N) { const u32x4* p = reinterpret_cast<const u32x4*>(table + off[i % F] + (i / F) * SEG); r[i % DEPTH] = NT ? __builtin_nontemporal_load(p) : *p; }
        __builtin_amdgcn_sched_barrier(0);
    }
    asm volatile("" :: "v"(pf));
    t -= q0 + q1;
#pragma unroll
    for (int m = 1; m < LPS; m <<= 1) t += __shfl_xor(t, m, 64);
    if (c == 0 && valid) out[b] = __float2bfloat16(0.5f * t);
}
extern "C" void kernel_launch(void* const* d_in, const int* in_sizes, int n_in, void* d_out, int out_size, void* d_ws, size_t ws_size, hipStream_t stream) {
    const int B = out_size; const int V = in_sizes[1] / 128;
    constexpr int SPB = 4 * 64 / (128 / 16);
    fm_kernel<128, 8, 0><<<(B + SPB - 1) / SPB, 256, 0, stream>>>((const int*)d_in[0], (const char*)d_in[1], (__hip_bfloat16*)d_out, B, V);
}
```

```cpp
#include <hip/hip_runtime.h>
#include <hip/hip_bf16.h>
#include <stdint.h>
typedef __attribute__((ext_vector_type(4))) unsigned int u32x4;
typedef __attribute__((ext_vector_type(2))) int i32x2;
constexpr int F = 26;
__device__ __forceinline__ float lo_f(unsigned u) { return __uint_as_float(u << 16); }
__device__ __forceinline__ float hi_f(unsigned u) { return __uint_as_float(u & 0xffff0000u); }
template <int SEG, int DEPTH, int NT>
__global__ __launch_bounds__(256) void fm_kernel(const int* __restrict__ idx, const char* __restrict__ table, __hip_bfloat16* __restrict__ out, int B, int V) {
    constexpr int PASSES = 256 / SEG, LPS = SEG / 16, SPW = 64 / LPS;
    const unsigned long long T0 = __builtin_amdgcn_s_memrealtime();
    __builtin_amdgcn_sched_barrier(0);
    const int lane = threadIdx.x & 63, w = threadIdx.x >> 6;
    int b = (blockIdx.x * 4 + w) * SPW + lane / LPS;
    const bool valid = b < B; if (!valid) b = B - 1;
    const int c = lane % LPS;
    unsigned off[F];
    const i32x2* ip = (const i32x2*)(idx + (size_t)b * F);
#pragma unroll
    for (int j = 0; j < F / 2; ++j) { i32x2 v = ip[j]; off[2 * j] = min((unsigned)v.x, (unsigned)V - 1) * 256u + c * 16; off[2 * j + 1] = min((unsigned)v.y, (unsigned)V - 1) * 256u + c * 16; }
    float t = 0.f;
    u32x4 r[DEPTH];
    float s[8]; float q0 = 0.f, q1 = 0.f;
#pragma unroll
    for (int j = 0; j < 8; ++j) s[j] = 0.f;
    constexpr int N = F * PASSES;
    asm volatile("s_waitcnt vmcnt(0)" ::: "memory");
    __builtin_amdgcn_sched_barrier(0);
    const unsigned long long T1 = __builtin_amdgcn_s_memrealtime(); unsigned long long T2 = 0, T3 = 0;
    __builtin_amdgcn_sched_barrier(0);
#pragma unroll
    for (int i = 0; i < N + DEPTH; ++i) {
        if (i >= DEPTH) {
            const int j = i - DEPTH;
            u32x4 x = r[j % DEPTH];
#pragma unroll
            for (int kk = 0; kk < 4; ++kk) { float a = lo_f(x[kk]), h = hi_f(x[kk]); s[2 * kk] += a; s[2 * kk + 1] += h; q0 = fmaf(a, a, q0); q1 = fmaf(h, h, q1); }
            if (j % F == F - 1) {
#pragma unroll
                for (int jj = 0; jj < 8; ++jj) { t = fmaf(s[jj], s[jj], t); s[jj] = 0.f; }
            }
        }
        if (i == DEPTH) { __builtin_amdgcn_sched_barrier(0); T2 = __builtin_amdgcn_s_memrealtime(); __builtin_amdgcn_sched_barrier(0); }
        if (i == DEPTH + F) { __builtin_amdgcn_sched_barrier(0); T3 = __builtin_amdgcn_s_memrealtime(); __builtin_amdgcn_sched_barrier(0); }
        if (i < N) { const u32x4* p = reinterpret_cast<const u32x4*>(table + off[i % F] + (i / F) * SEG); r[i % DEPTH] = NT ? __builtin_nontemporal_load(p) : *p; }
        __builtin_amdgcn_sched_barrier(0);
    }
    t -= q0 + q1;
#pragma unroll
    for (int m = 1; m < LPS; m <<= 1) t += __shfl_xor(t, m, 64);
    if (c == 0 && valid) out[b] = __float2bfloat16(0.5f * t);
    const unsigned long long T4 = __builtin_amdgcn_s_memrealtime();
    const unsigned long long Tend = T0 + (4000);
    while (__builtin_amdgcn_s_memrealtime() < Tend) __builtin_amdgcn_s_sleep(2);
}
extern "C" void kernel_launch(void* const* d_in, const int* in_sizes, int n_in, void* d_out, int out_size, void* d_ws, size_t ws_size, hipStream_t stream) {
    const int B = out_size; const int V = in_sizes[1] / 128;
    constexpr int SPB = 4 * 64 / (128 / 16);
    fm_kernel<128, 8, 0><<<(B + SPB - 1) / SPB, 256, 0, stream>>>((const int*)d_in[0], (const char*)d_in[1], (__hip_bfloat16*)d_out, B, V);
}
```

```cpp
#include <hip/hip_runtime.h>
#include <hip/hip_bf16.h>
#include <stdint.h>

typedef __attribute__((ext_vector_type(4))) unsigned int u32x4;
typedef __attribute__((ext_vector_type(2))) unsigned int u32x2;
typedef __attribute__((ext_vector_type(4))) int i32x4;
typedef __attribute__((address_space(1))) unsigned long long gu64;
typedef __attribute__((address_space(1))) unsigned int gu32;

#define WPB 16
constexpr int F = 26;
constexpr unsigned TAG_STEP = 0x9E3779B1u;

template <int CTRL>
__device__ __forceinline__ unsigned dpp(unsigned v) {
    return (unsigned)__builtin_amdgcn_update_dpp(0, (int)v, CTRL, 0xf, 0xf, true);
}
template <int CTRL>
__device__ __forceinline__ float dppf(float v) { return __uint_as_float(dpp<CTRL>(__float_as_uint(v))); }
constexpr int DPP_XOR1 = 0xB1;
constexpr int DPP_XOR2 = 0x4E;
constexpr int DPP_HMIR = 0x141;

struct Acc { float s[8]; float q0, q1; };
__device__ __forceinline__ void unpack(const u32x4 x, float (&f)[8]) {
#pragma unroll
    for (int k = 0; k < 4; ++k) { f[2 * k] = __uint_as_float(x[k] << 16); f[2 * k + 1] = __uint_as_float(x[k] & 0xffff0000u); }
}
__device__ __forceinline__ void accum(Acc& a, const float (&f)[8]) {
#pragma unroll
    for (int k = 0; k < 4; ++k) { a.s[2 * k] += f[2 * k]; a.s[2 * k + 1] += f[2 * k + 1]; a.q0 = fmaf(f[2 * k], f[2 * k], a.q0); a.q1 = fmaf(f[2 * k + 1], f[2 * k + 1], a.q1); }
}

__global__ __launch_bounds__(WPB * 64) void fm_kernel(const int* __restrict__ idx, const char* __restrict__ table,
                                                      __hip_bfloat16* __restrict__ out, unsigned long long* slots,
                                                      unsigned* epochs, int B, int V, unsigned bmul) {
    __shared__ __attribute__((aligned(16))) unsigned lists[WPB * 8 * 32];
    const int lane = threadIdx.x & 63, w = threadIdx.x >> 6;
    const unsigned h = (blockIdx.x >> 2) & 1;
    const int g = ((blockIdx.x >> 3) * 4 + (blockIdx.x & 3)) * WPB + w;
    if (g * 8 >= B) return;
    const int j = lane & 7;
    const int bq = g * 8 + (lane >> 3);
    const bool valid = bq < B;
    const int b = valid ? bq : B - 1;
    const i32x4 iv = *reinterpret_cast<const i32x4*>(reinterpret_cast<const char*>(idx) + (size_t)b * (F * 4) + min(16 * j, 88));
    const unsigned ep = __hip_atomic_load((gu32*)(epochs + g), __ATOMIC_RELAXED, __HIP_MEMORY_SCOPE_AGENT);
    unsigned* mylist = lists + (w * 8 + (lane >> 3)) * 32;
    const unsigned colo = h * 128 + j * 16;
    {
        unsigned row[4], sh[4], oh[4];
#pragma unroll
        for (int k = 0; k < 4; ++k) {
            int r = iv[k];
            r = r < 0 ? r + V : r;
            row[k] = min((unsigned)r, (unsigned)(V - 1));
            sh[k] = (__umul24(row[k], bmul) >> 21) & 0x18;
            const unsigned ok = (j < 6 || (j == 6 && k >= 2)) ? 1u : 0u;
            oh[k] = ok << sh[k];
        }
        unsigned sum = oh[0] + oh[1] + oh[2] + oh[3], pre = 0;
        unsigned y;
        y = dpp<DPP_XOR1>(sum); pre += (j & 1) ? y : 0; sum += y;
        y = dpp<DPP_XOR2>(sum); pre += (j & 2) ? y : 0; sum += y;
        y = dpp<DPP_HMIR>(sum); pre += (j & 4) ? y : 0; sum += y;
        unsigned start = pre + (sum << 8) + (sum << 16) + (sum << 24);
#pragma unroll
        for (int k = 0; k < 4; ++k) {
            unsigned pos = (start >> sh[k]) & 0xff;
            pos = oh[k] ? pos : 28 + k;
            mylist[pos] = row[k] << 8;
            start += oh[k];
        }
    }
    __builtin_amdgcn_fence(__ATOMIC_ACQ_REL, "wavefront");
    __builtin_amdgcn_wave_barrier();
    const u32x2* lp = reinterpret_cast<const u32x2*>(mylist);
    u32x2 o = lp[0];
    u32x2 on = lp[1];
    u32x4 r0 = *reinterpret_cast<const u32x4*>(table + (o.x + colo));
    u32x4 r1 = *reinterpret_cast<const u32x4*>(table + (o.y + colo));
    Acc a;
#pragma unroll
    for (int k = 0; k < 8; ++k) a.s[k] = 0.f;
    a.q0 = 0.f; a.q1 = 0.f;
    float f[8];
#pragma unroll 1
    for (int it = 0; it < F / 2 - 1; ++it) {
        o = on;
        on = lp[it + 2];
        const unsigned a0 = o.x + colo, a1 = o.y + colo;
        __builtin_amdgcn_sched_barrier(0);
        unpack(r0, f);
        __builtin_amdgcn_sched_barrier(0);
        r0 = *reinterpret_cast<const u32x4*>(table + a0);
        __builtin_amdgcn_sched_barrier(0);
        accum(a, f);
        __builtin_amdgcn_sched_barrier(0);
        unpack(r1, f);
        __builtin_amdgcn_sched_barrier(0);
        r1 = *reinterpret_cast<const u32x4*>(table + a1);
        __builtin_amdgcn_sched_barrier(0);
        accum(a, f);
        __builtin_amdgcn_sched_barrier(0);
    }
    unpack(r0, f); accum(a, f);
    unpack(r1, f); accum(a, f);
    float t = -(a.q0 + a.q1);
#pragma unroll
    for (int k = 0; k < 8; ++k) t = fmaf(a.s[k], a.s[k], t);
    t += dppf<DPP_XOR1>(t);
    t += dppf<DPP_XOR2>(t);
    t += dppf<DPP_HMIR>(t);
    if (j == 0 && valid) {
        const unsigned tag = ep + TAG_STEP;
        const unsigned long long mine = ((unsigned long long)tag << 32) | __float_as_uint(t);
        const unsigned long long old = __hip_atomic_exchange((gu64*)(slots + b), mine, __ATOMIC_RELAXED, __HIP_MEMORY_SCOPE_AGENT);
        if ((unsigned)(old >> 32) == tag) {
            out[b] = __float2bfloat16(0.5f * (t + __uint_as_float((unsigned)old)));
            __hip_atomic_store((gu32*)(epochs + g), tag, __ATOMIC_RELAXED, __HIP_MEMORY_SCOPE_AGENT);
        }
    }
}

extern "C" void kernel_launch(void* const* d_in, const int* in_sizes, int n_in, void* d_out, int out_size, void* d_ws,
                              size_t ws_size, hipStream_t stream) {
    const int B = out_size;
    const int V = in_sizes[1] / 128;
    const int G = (B + 7) / 8;
    const int nb = (G + 4 * WPB - 1) / (4 * WPB);
    unsigned long long* slots = (unsigned long long*)d_ws;
    unsigned* epochs = (unsigned*)((char*)d_ws + (size_t)G * 64);
    const unsigned bmul = (unsigned)(((unsigned long long)4 << 24) / (unsigned)V);
    fm_kernel<<<nb * 8, WPB * 64, 0, stream>>>((const int*)d_in[0], (const char*)d_in[1], (__hip_bfloat16*)d_out, slots, epochs, B, V,
                                               bmul > 0xffffffu ? 0xffffffu : bmul);
}
```

```cpp
#include <hip/hip_runtime.h>
#include <hip/hip_bf16.h>
#include <stdint.h>

typedef __attribute__((ext_vector_type(4))) unsigned int u32x4;
typedef __attribute__((ext_vector_type(2))) float f32x2;
typedef __attribute__((address_space(1))) unsigned long long gu64;
typedef __attribute__((address_space(1))) unsigned int gu32;
typedef __attribute__((address_space(1))) unsigned short gu16;

constexpr int F = 26;
constexpr int WPB = 8;
constexpr int NG = 2;
constexpr int DEPTH = 3;
constexpr int NIT = 7;
constexpr int TB = F - DEPTH * (NIT + 1);
constexpr int PEEK_AT = 6;
constexpr unsigned TAG_STEP = 0x9E3779B1u;

template <int CTRL>
__device__ __forceinline__ unsigned dpp(unsigned v) {
    return (unsigned)__builtin_amdgcn_update_dpp(0, (int)v, CTRL, 0xf, 0xf, true);
}
template <int CTRL>
__device__ __forceinline__ float dppf(float v) { return __uint_as_float(dpp<CTRL>(__float_as_uint(v))); }
constexpr int DPP_XOR1 = 0xB1;
constexpr int DPP_XOR2 = 0x4E;
constexpr int DPP_HMIR = 0x141;

struct Acc {
    f32x2 s[4];
    f32x2 q;
};
__device__ __forceinline__ void unpack(const u32x4 x, f32x2 (&f)[4]) {
#pragma unroll
    for (int k = 0; k < 4; ++k) {
        f[k].x = __uint_as_float(x[k] << 16);
        f[k].y = __uint_as_float(x[k] & 0xffff0000u);
    }
    asm volatile("" : "+v"(f[0]), "+v"(f[1]), "+v"(f[2]), "+v"(f[3]));
}
__device__ __forceinline__ void accum(Acc& a, const f32x2 (&f)[4]) {
#pragma unroll
    for (int k = 0; k < 4; ++k) {
        a.s[k] += f[k];
        a.q = __builtin_elementwise_fma(f[k], f[k], a.q);
    }
    asm volatile("" : "+v"(a.s[0]), "+v"(a.s[1]), "+v"(a.s[2]), "+v"(a.s[3]), "+v"(a.q));
}
__device__ __forceinline__ unsigned mulhi24(unsigned a, unsigned b) {
    unsigned r;
    asm("v_mul_hi_u32_u24 %0, %1, %2" : "=v"(r) : "s"(b), "v"(a));
    return r;
}
#define SCHED_FENCE() __builtin_amdgcn_sched_barrier(0)

__global__ __launch_bounds__(WPB * 64) void fm_kernel(const int* __restrict__ idx, const char* __restrict__ table,
                                                      __hip_bfloat16* __restrict__ out, char* ws, int B, int V,
                                                      unsigned bmul, unsigned epoch_off) {
    __shared__ __attribute__((aligned(16))) unsigned lists[WPB * NG * 8 * 32];
    const int lane = threadIdx.x & 63, w = threadIdx.x >> 6;
    const unsigned h = ((blockIdx.x >> 2) ^ blockIdx.x) & 1;
    const int task = ((blockIdx.x >> 3) * 4 + (blockIdx.x & 3)) * WPB + w;
    const int j = lane & 7;

    int b[NG];
    bool valid[NG];
    u32x4 iv[NG];
#pragma unroll
    for (int n = 0; n < NG; ++n) {
        const int bq = (task * NG + n) * 8 + (lane >> 3);
        valid[n] = bq < B;
        b[n] = valid[n] ? bq : B - 1;
        iv[n] = *reinterpret_cast<const u32x4*>(reinterpret_cast<const char*>(idx) +
                                                ((unsigned)b[n] * (F * 4) + min(16 * j, 88)));
    }
    gu32* epp = (gu32*)(ws + (epoch_off + (unsigned)task * 4u));
    const unsigned ep = __hip_atomic_load(epp, __ATOMIC_RELAXED, __HIP_MEMORY_SCOPE_AGENT);

    const __amdgpu_buffer_rsrc_t trs =
        __builtin_amdgcn_make_buffer_rsrc((void*)table, 0, (unsigned)V * 256u, 0x00020000);
    unsigned* mylist = lists + (w * NG * 8 + (lane >> 3)) * 32;
    const unsigned colo = h * 128 + j * 16;
    const unsigned ok01 = j < 6 ? 1u : 0u, ok23 = j < 7 ? 1u : 0u;
    const unsigned m1 = (j & 1) ? ~0u : 0u, m2 = (j & 2) ? ~0u : 0u, m4 = (j & 4) ? ~0u : 0u;
    const unsigned dead01 = j < 6 ? 0u : 28u, dead23 = j < 7 ? 0u : 28u;
    auto ldrow = [&](unsigned row) { return __builtin_amdgcn_raw_buffer_load_b128(trs, (row << 8) + colo, 0, 0); };

    u32x4 r[NG][DEPTH];
    unsigned on[NG][DEPTH];
    SCHED_FENCE();
#pragma unroll
    for (int n = 0; n < NG; ++n) {
        unsigned row[4], sh[4], oh[4];
#pragma unroll
        for (int k = 0; k < 4; ++k) {
            row[k] = min(iv[n][k], iv[n][k] + (unsigned)V);
            sh[k] = mulhi24(row[k], bmul) * 5u;
            oh[k] = (k < 2 ? ok01 : ok23) << sh[k];
        }
        unsigned sum = oh[0] + oh[1] + oh[2] + oh[3], pre;
        pre = dpp<DPP_XOR1>(sum) & m1;
        sum += dpp<DPP_XOR1>(sum);
        pre += dpp<DPP_XOR2>(sum) & m2;
        sum += dpp<DPP_XOR2>(sum);
        pre += dpp<DPP_HMIR>(sum) & m4;
        sum += dpp<DPP_HMIR>(sum);
        unsigned start = pre + sum * 0x02108420u;
#pragma unroll
        for (int k = 0; k < 4; ++k) {
            const unsigned pos = ((start >> sh[k]) & 31u) | (k < 2 ? dead01 : dead23);
            mylist[n * 256 + pos] = row[k];
            start += oh[k];
        }
        __builtin_amdgcn_fence(__ATOMIC_ACQ_REL, "wavefront");
        __builtin_amdgcn_wave_barrier();
#pragma unroll
        for (int k = 0; k < DEPTH; ++k) r[n][k] = ldrow(mylist[n * 256 + k]);
#pragma unroll
        for (int k = 0; k < DEPTH; ++k) on[n][k] = mylist[n * 256 + DEPTH + k];
    }

    Acc a[NG];
#pragma unroll
    for (int n = 0; n < NG; ++n) {
#pragma unroll
        for (int k = 0; k < 4; ++k) a[n].s[k] = (f32x2){0.f, 0.f};
        a[n].q = (f32x2){0.f, 0.f};
    }
    f32x2 f[4];
    SCHED_FENCE();
#pragma unroll 1
    for (int it = 0; it < NIT; ++it) {
        unsigned o[NG][DEPTH];
#pragma unroll
        for (int n = 0; n < NG; ++n)
#pragma unroll
            for (int k = 0; k < DEPTH; ++k) {
                o[n][k] = on[n][k];
                on[n][k] = mylist[n * 256 + DEPTH * (it + 2) + k];
            }
        SCHED_FENCE();
#pragma unroll
        for (int n = 0; n < NG; ++n)
#pragma unroll
            for (int k = 0; k < DEPTH; ++k) {
                unpack(r[n][k], f);
                SCHED_FENCE();
                r[n][k] = ldrow(o[n][k]);
                SCHED_FENCE();
                accum(a[n], f);
                SCHED_FENCE();
            }
    }

    const int bh = j == 1 ? b[1] : b[0];
    const bool vh = j == 1 ? valid[1] : valid[0];
    gu64* slot = (gu64*)(ws + (unsigned)bh * 8u);
    unsigned long long peek = 0;
    {
        u32x4 rt[NG][TB];
#pragma unroll
        for (int n = 0; n < NG; ++n)
#pragma unroll
            for (int k = 0; k < TB; ++k) rt[n][k] = ldrow(on[n][k]);
        SCHED_FENCE();
        constexpr int NT = NG * (DEPTH + TB);
#pragma unroll
        for (int i = 0; i < NT; ++i) {
            if (NT - i == PEEK_AT) {
                peek = __hip_atomic_load(slot, __ATOMIC_RELAXED, __HIP_MEMORY_SCOPE_AGENT);
                SCHED_FENCE();
            }
            const bool first = i < NG * DEPTH;
            const int n = first ? i / DEPTH : (i - NG * DEPTH) / TB;
            const int k = first ? i % DEPTH : (i - NG * DEPTH) % TB;
            unpack(first ? r[n][k] : rt[n][k], f);
            accum(a[n], f);
            SCHED_FENCE();
        }
    }
    float th = 0.f;
#pragma unroll
    for (int n = 0; n < NG; ++n) {
        f32x2 t2 = -a[n].q;
#pragma unroll
        for (int k = 0; k < 4; ++k) t2 = __builtin_elementwise_fma(a[n].s[k], a[n].s[k], t2);
        float t = t2.x + t2.y;
        t += dppf<DPP_XOR1>(t);
        t += dppf<DPP_XOR2>(t);
        t += dppf<DPP_HMIR>(t);
        th = (n == 0 || j == n) ? t : th;
    }
    if (j < NG && vh) {
        const unsigned tag = ep + TAG_STEP;
        unsigned long long other = peek;
        if ((unsigned)(other >> 32) != tag) {
            const unsigned long long mine = ((unsigned long long)tag << 32) | __float_as_uint(th);
            other = __hip_atomic_exchange(slot, mine, __ATOMIC_RELAXED, __HIP_MEMORY_SCOPE_AGENT);
        }
        if ((unsigned)(other >> 32) == tag) {
            const __hip_bfloat16 res = __float2bfloat16(0.5f * (th + __uint_as_float((unsigned)other)));
            __hip_atomic_store((gu16*)(out + bh), __builtin_bit_cast(unsigned short, res), __ATOMIC_RELAXED,
                               __HIP_MEMORY_SCOPE_AGENT);
            __hip_atomic_store(epp, tag, __ATOMIC_RELAXED, __HIP_MEMORY_SCOPE_AGENT);
        }
    }
}

extern "C" void kernel_launch(void* const* d_in, const int* in_sizes, int n_in, void* d_out, int out_size, void* d_ws,
                              size_t ws_size, hipStream_t stream) {
    const int B = out_size;
    const int V = in_sizes[1] / 128;
    const int tasks = (B + 8 * NG - 1) / (8 * NG);
    const int nb = (tasks + 4 * WPB - 1) / (4 * WPB);
    const unsigned long long bm = ((unsigned long long)6 << 32) / (unsigned)V;
    fm_kernel<<<nb * 8, WPB * 64, 0, stream>>>((const int*)d_in[0], (const char*)d_in[1], (__hip_bfloat16*)d_out,
                                               (char*)d_ws, B, V, bm > 0xffffffull ? 0xffffffu : (unsigned)bm,
                                               (unsigned)tasks * (8u * NG * 8u));
}
```
